```python
import torch

def _softmax(x, axis=-1):
    return torch.softmax(x, dim=axis)
def _gelu(x):
    return torch.nn.functional.gelu(x, approximate="tanh")
def _silu(x):
    return torch.nn.functional.silu(x)
def _sigmoid(x):
    return torch.sigmoid(x)
def _layernorm(x, axis=-1, eps=1e-5):
    m = x.mean(dim=axis, keepdim=True)
    v = x.var(dim=axis, keepdim=True, unbiased=False)
    return (x - m) / torch.sqrt(v + eps)
def _scatter_add(tgt, idx, src, axis=0):
    return tgt.clone().index_add_(axis, idx, src)
def _segment_sum(src, seg, n):
    out = torch.zeros((n,) + src.shape[1:], dtype=src.dtype)
    return out.index_add_(0, seg, src)
def _pad(x, pw):
    flat = tuple(v for pair in reversed(pw) for v in pair)
    return torch.nn.functional.pad(x, flat)

kernel_name = 'sequential_preLN__sliding_block_cached__moe_arch_mod__decode'

def setup_inputs():

    _w = (lambda a: (lambda t: t.to(torch.bfloat16) if t.is_floating_point() else t)(torch.as_tensor(a)))
    def _A_setup():
        B, Sq, D = 8, 1, 576
        Hq, Hkv, Sk, Dh = 32, 16, 4096, 128
        assert Hq % Hkv == 0, (Hq, Hkv)
        return {
            "x": _w((lambda *s: torch.randn(*s, dtype=torch.bfloat16) * 0.05)(B, Sq, D)),
            "k_cache": _w((lambda *s: torch.randn(*s, dtype=torch.bfloat16) * 3.2)(B, Hkv, Sk, Dh)),
            "v_cache": _w((lambda *s: torch.randn(*s, dtype=torch.bfloat16) * 0.4)(B, Hkv, Sk, Dh)),
            "wq": _w((lambda *s: torch.randn(*s, dtype=torch.bfloat16) * 3.2)(D, Hq * Dh) * (D ** -0.5)),
            "wo": _w((lambda *s: torch.randn(*s, dtype=torch.bfloat16) * 0.4)(Hq * Dh, D) * ((Hq * Dh) ** -0.5)),
        }
    _A_in = _A_setup()

    def _B_setup():
        B, S, D, F, C = 8, 1, 576, 4608, 1
        return {
            "x": _w((lambda *s: torch.randn(*s, dtype=torch.bfloat16) * 0.05)(B, S, D)),
            "w_r": _w((lambda *s: torch.randn(*s, dtype=torch.bfloat16) * 0.4)(D)),
            "w_gate": _w((lambda *s: torch.randn(*s, dtype=torch.bfloat16) * 0.05)(D, F)),
            "w_up": _w((lambda *s: torch.randn(*s, dtype=torch.bfloat16) * 0.05)(D, F)),
            "w_down": _w((lambda *s: torch.randn(*s, dtype=torch.bfloat16) * 0.05)(F, D)),
        }
    _B_in = _B_setup()

    return {
        "A_x": _A_in["x"],
        "A_k_cache": _A_in["k_cache"],
        "A_v_cache": _A_in["v_cache"],
        "A_wq": _A_in["wq"],
        "A_wo": _A_in["wo"],
        "B_x": _B_in["x"],
        "B_w_r": _B_in["w_r"],
        "B_w_gate": _B_in["w_gate"],
        "B_w_up": _B_in["w_up"],
        "B_w_down": _B_in["w_down"],
    }

def reference(A_x, A_k_cache, A_v_cache, A_wq, A_wo, B_x, B_w_r, B_w_gate, B_w_up, B_w_down):
    def _A_ref(x, k_cache, v_cache, wq, wo):
        B, Sq, D = x.shape
        Hq, Dh = 32, 128
        Hkv, Sk = k_cache.shape[1], k_cache.shape[2]
        W = min(4096, max(256, Sk // 4))
        rep = Hq // Hkv
        q = (lambda x, *d: x.permute(*d))((x @ wq).reshape(B, Sq, Hq, Dh), 0, 2, 1, 3)
        k = (lambda x, n, axis: torch.repeat_interleave(x, n, dim=axis))(k_cache[:, :, Sk - W:, :], rep, axis=1)
        v = (lambda x, n, axis: torch.repeat_interleave(x, n, dim=axis))(v_cache[:, :, Sk - W:, :], rep, axis=1)
        scores = torch.einsum("bhqd,bhkd->bhqk", q, k) * (Dh ** -0.5)
        p = _softmax(scores, axis=-1)
        o = torch.einsum("bhqk,bhkd->bhqd", p, v)
        return (lambda x, *d: x.permute(*d))(o, 0, 2, 1, 3).reshape(B, Sq, Hq * Dh) @ wo

    def _B_ref(x, w_r, w_gate, w_up, w_down):
        B, S, D = x.shape
        C = 1
        r = x @ w_r
        idx = torch.argsort(-r, axis=-1)[:, :C]
        idx_e = (lambda x, n, axis: torch.repeat_interleave(x, n, dim=axis))(idx.reshape(B, C, 1), D, axis=2)
        x_sel = (lambda x, i, axis: torch.take_along_dim(x, i, dim=axis))(x, idx_e, axis=1)
        g = _silu(x_sel @ w_gate)
        u = x_sel @ w_up
        y = (g * u) @ w_down
        r_sel = _sigmoid((lambda x, i, axis: torch.take_along_dim(x, i, dim=axis))(r, idx, axis=1))
        y = y * r_sel.reshape(B, C, 1)
        b_off = (torch.arange(B) * S).reshape(B, 1)
        flat_idx = (idx + b_off).reshape(B * C)
        zero = (x * 0.0).reshape(B * S, D)
        upd = _scatter_add(zero, flat_idx, y.reshape(B * C, D), axis=0)
        return x + upd.reshape(B, S, D)

    x = A_x
    h = x + _A_ref(_layernorm(x, axis=-1), A_k_cache, A_v_cache, A_wq, A_wo)
    h2 = h + _B_ref(_layernorm(h, axis=-1), B_w_r, B_w_gate, B_w_up, B_w_down)
    return h2
```

```cpp
#include <hip/hip_runtime.h>
#include <hip/hip_bf16.h>
#include <stdint.h>

typedef unsigned short u16;
typedef unsigned v4u __attribute__((ext_vector_type(4)));

constexpr int B = 8, D = 576, HQ = 32, HKV = 16, SK = 4096, DH = 128, W = 1024, F = 4608;

__device__ __forceinline__ float bf2f(u16 b) { return __builtin_bit_cast(float, (unsigned)b << 16); }
__device__ __forceinline__ float rbf(float f) {
  unsigned u = __builtin_bit_cast(unsigned, f);
  u += 0x7FFFu + ((u >> 16) & 1u);
  return __builtin_bit_cast(float, u & 0xFFFF0000u);
}
__device__ __forceinline__ u16 f2bf(float f) {
  unsigned u = __builtin_bit_cast(unsigned, f);
  u += 0x7FFFu + ((u >> 16) & 1u);
  return (u16)(u >> 16);
}
__device__ __forceinline__ float wsum(float v) {
  for (int o = 32; o > 0; o >>= 1) v += __shfl_xor(v, o);
  return v;
}
__device__ __forceinline__ float wmax(float v) {
  for (int o = 32; o > 0; o >>= 1) v = fmaxf(v, __shfl_xor(v, o));
  return v;
}

__device__ void ln8(const float* in  , float* xn  , int tid, int nthr) {
  int lane = tid & 63, w = tid >> 6, nw = nthr >> 6;
  for (int b = w; b < B; b += nw) {
    float s = 0.f;
    for (int i = lane; i < D; i += 64) s += in[b * D + i];
    s = wsum(s);
    float mean = s * (1.0f / D);
    float s2 = 0.f;
    for (int i = lane; i < D; i += 64) { float d = in[b * D + i] - mean; s2 += d * d; }
    s2 = wsum(s2);
    float var = s2 * (1.0f / D);
    float m = rbf(mean), v = rbf(var);
    float den = rbf(sqrtf(rbf(v + 1e-5f)));
    for (int i = lane; i < D; i += 64) xn[b * D + i] = rbf(rbf(in[b * D + i] - m) / den);
  }
}

__global__ void k_q(const u16* x, const u16* wq, float* q) {
  __shared__ float xs[B * D], xn[B * D], red[4][B][64];
  int tid = threadIdx.x;
  for (int i = tid; i < B * D; i += 256) xs[i] = bf2f(x[i]);
  __syncthreads();
  ln8(xs, xn, tid, 256);
  __syncthreads();
  int c = blockIdx.x * 64 + (tid & 63), kp = tid >> 6;
  float acc[B];
  for (int b = 0; b < B; ++b) acc[b] = 0.f;
  for (int k = kp * 144; k < kp * 144 + 144; ++k) {
    float w = bf2f(wq[(size_t)k * (HQ * DH) + c]);
    for (int b = 0; b < B; ++b) acc[b] = fmaf(xn[b * D + k], w, acc[b]);
  }
  for (int b = 0; b < B; ++b) red[kp][b][tid & 63] = acc[b];
  __syncthreads();
  for (int i = tid; i < B * 64; i += 256) {
    int b = i >> 6, cc = i & 63;
    float s = red[0][b][cc] + red[1][b][cc] + red[2][b][cc] + red[3][b][cc];
    q[b * (HQ * DH) + blockIdx.x * 64 + cc] = rbf(s);
  }
}

__global__ void k_attn(const float* q, const u16* kc, const u16* vc, float* o) {
  __shared__ float qs[DH], sc[W], red[8], ors[4][DH];
  int hq = blockIdx.x, b = blockIdx.y, tid = threadIdx.x, lane = tid & 63, w = tid >> 6;
  int hk = hq >> 1;
  if (tid < DH) qs[tid] = q[b * (HQ * DH) + hq * DH + tid];
  __syncthreads();
  const u16* kb = kc + ((size_t)(b * HKV + hk) * SK + (SK - W)) * DH;
  const u16* vb = vc + ((size_t)(b * HKV + hk) * SK + (SK - W)) * DH;
  float mx = -INFINITY;
  for (int k = tid; k < W; k += 256) {
    float s = 0.f;
    for (int d = 0; d < DH; ++d) s = fmaf(qs[d], bf2f(kb[(size_t)k * DH + d]), s);
    s = rbf(rbf(s) * 0.08838834764831845f);
    sc[k] = s; mx = fmaxf(mx, s);
  }
  mx = wmax(mx);
  if (lane == 0) red[w] = mx;
  __syncthreads();
  mx = fmaxf(fmaxf(red[0], red[1]), fmaxf(red[2], red[3]));
  float sum = 0.f;
  for (int k = tid; k < W; k += 256) { float e = __expf(sc[k] - mx); sc[k] = e; sum += e; }
  sum = wsum(sum);
  if (lane == 0) red[4 + w] = sum;
  __syncthreads();
  sum = red[4] + red[5] + red[6] + red[7];
  for (int k = tid; k < W; k += 256) sc[k] = rbf(sc[k] / sum);
  __syncthreads();
  float a0 = 0.f, a1 = 0.f;
  for (int k = w * 256; k < w * 256 + 256; ++k) {
    unsigned vv = *(const unsigned*)(vb + (size_t)k * DH + 2 * lane);
    float p = sc[k];
    a0 = fmaf(p, bf2f(vv & 0xffff), a0); a1 = fmaf(p, bf2f(vv >> 16), a1);
  }
  ors[w][2 * lane] = a0; ors[w][2 * lane + 1] = a1;
  __syncthreads();
  if (tid < DH) o[b * (HQ * DH) + hq * DH + tid] = rbf(ors[0][tid] + ors[1][tid] + ors[2][tid] + ors[3][tid]);
}

__global__ void k_wo(const float* o, const u16* wo, const u16* x, float* h) {
  __shared__ float red[16][B][64];
  int tid = threadIdx.x, c = blockIdx.x * 64 + (tid & 63), kp = tid >> 6;
  float acc[B];
  for (int b = 0; b < B; ++b) acc[b] = 0.f;
  for (int k = kp * 256; k < kp * 256 + 256; ++k) {
    float w = bf2f(wo[(size_t)k * D + c]);
    for (int b = 0; b < B; ++b) acc[b] = fmaf(o[b * 4096 + k], w, acc[b]);
  }
  for (int b = 0; b < B; ++b) red[kp][b][tid & 63] = acc[b];
  __syncthreads();
  for (int i = tid; i < B * 64; i += 1024) {
    int b = i >> 6, cc = i & 63;
    float s = 0.f;
    for (int j = 0; j < 16; ++j) s += red[j][b][cc];
    int col = blockIdx.x * 64 + cc;
    h[b * D + col] = rbf(bf2f(x[b * D + col]) + rbf(s));
  }
}

__global__ void k_gu(const float* h, const u16* wg, const u16* wu, float* a) {
  __shared__ float xs[B * D], xn[B * D], red[2][4][B][64];
  int tid = threadIdx.x;
  for (int i = tid; i < B * D; i += 256) xs[i] = h[i];
  __syncthreads();
  ln8(xs, xn, tid, 256);
  __syncthreads();
  int c = blockIdx.x * 64 + (tid & 63), kp = tid >> 6;
  float ag[B], au[B];
  for (int b = 0; b < B; ++b) ag[b] = au[b] = 0.f;
  for (int k = kp * 144; k < kp * 144 + 144; ++k) {
    float g = bf2f(wg[(size_t)k * F + c]), u = bf2f(wu[(size_t)k * F + c]);
    for (int b = 0; b < B; ++b) { ag[b] = fmaf(xn[b * D + k], g, ag[b]); au[b] = fmaf(xn[b * D + k], u, au[b]); }
  }
  for (int b = 0; b < B; ++b) { red[0][kp][b][tid & 63] = ag[b]; red[1][kp][b][tid & 63] = au[b]; }
  __syncthreads();
  for (int i = tid; i < B * 64; i += 256) {
    int b = i >> 6, cc = i & 63;
    float g = rbf(red[0][0][b][cc] + red[0][1][b][cc] + red[0][2][b][cc] + red[0][3][b][cc]);
    float u = rbf(red[1][0][b][cc] + red[1][1][b][cc] + red[1][2][b][cc] + red[1][3][b][cc]);
    g = rbf(g / (1.0f + __expf(-g)));
    a[b * F + blockIdx.x * 64 + cc] = rbf(g * u);
  }
}

__global__ void k_dn(const float* h, const float* a, const u16* wd, const u16* wr, u16* out) {
  __shared__ float xs[B * D], xn[B * D], red[16][B][64], rs[B];
  int tid = threadIdx.x;
  for (int i = tid; i < B * D; i += 1024) xs[i] = h[i];
  __syncthreads();
  ln8(xs, xn, tid, 1024);
  __syncthreads();
  if (tid < 512) {
    int b = tid >> 6, lane = tid & 63;
    float s = 0.f;
    for (int i = lane; i < D; i += 64) s = fmaf(xn[b * D + i], bf2f(wr[i]), s);
    s = wsum(s);
    s = rbf(s);
    if (lane == 0) rs[b] = rbf(1.0f / (1.0f + __expf(-s)));
  }
  int c = blockIdx.x * 64 + (tid & 63), kp = tid >> 6;
  float acc[B];
  for (int b = 0; b < B; ++b) acc[b] = 0.f;
  for (int k = kp * 288; k < kp * 288 + 288; ++k) {
    float w = bf2f(wd[(size_t)k * D + c]);
    for (int b = 0; b < B; ++b) acc[b] = fmaf(a[b * F + k], w, acc[b]);
  }
  for (int b = 0; b < B; ++b) red[kp][b][tid & 63] = acc[b];
  __syncthreads();
  for (int i = tid; i < B * 64; i += 1024) {
    int b = i >> 6, cc = i & 63;
    float s = 0.f;
    for (int j = 0; j < 16; ++j) s += red[j][b][cc];
    int col = blockIdx.x * 64 + cc;
    float y = rbf(rbf(s) * rs[b]);
    float bo = rbf(xn[b * D + col] + y);
    out[b * D + col] = f2bf(xs[b * D + col] + bo);
  }
}

extern "C" void kernel_launch(void* const* d_in, const int* in_sizes, int n_in,
                              void* d_out, int out_size, void* d_ws, size_t ws_size,
                              hipStream_t stream) {
  const u16* x = (const u16*)d_in[0];
  const u16* kc = (const u16*)d_in[1];
  const u16* vc = (const u16*)d_in[2];
  const u16* wq = (const u16*)d_in[3];
  const u16* wo = (const u16*)d_in[4];
  const u16* wr = (const u16*)d_in[6];
  const u16* wg = (const u16*)d_in[7];
  const u16* wu = (const u16*)d_in[8];
  const u16* wd = (const u16*)d_in[9];
  float* ws = (float*)d_ws;
  float* q = ws; float* o = ws + 32768; float* h = ws + 65536; float* a = ws + 81920;
  k_q<<<64, 256, 0, stream>>>(x, wq, q);
  k_attn<<<dim3(HQ, B), 256, 0, stream>>>(q, kc, vc, o);
  k_wo<<<9, 1024, 0, stream>>>(o, wo, x, h);
  k_gu<<<72, 256, 0, stream>>>(h, wg, wu, a);
  k_dn<<<9, 1024, 0, stream>>>(h, a, wd, wr, (u16*)d_out);
}
```

```cpp
#include <hip/hip_runtime.h>
#include <stdint.h>

namespace dk {
typedef unsigned short u16;
typedef unsigned u32;
typedef unsigned long long u64;
typedef u32 v4u __attribute__((ext_vector_type(4)));
typedef u32 v2u __attribute__((ext_vector_type(2)));
typedef float v4f __attribute__((ext_vector_type(4)));
typedef float v2f __attribute__((ext_vector_type(2)));
typedef short v4s __attribute__((ext_vector_type(4)));
typedef __bf16 v8bf __attribute__((ext_vector_type(8)));
typedef __bf16 v2bf __attribute__((ext_vector_type(2)));
#define GAS __attribute__((address_space(1)))
#define LAS __attribute__((address_space(3)))
typedef LAS unsigned char* lds_t;

constexpr int D = 576, SK = 4096, DH = 128, WIN = 1024;

constexpr int WS_EPOCH = 0;
constexpr int WS_Q = 4096;
constexpr int WS_O = WS_Q + 16 * 8 * 8192;
constexpr int WS_M = WS_O + 16 * 8 * 2 * 1024;
constexpr int WS_A = WS_M + 4096;
constexpr int WS_Bh = WS_A + 8 * 9 * 16 * 256;
constexpr int WS_G = WS_Bh + 8 * 18432;
constexpr int WS_C = WS_G + 36 * 4 * 8 * 1024;
constexpr int WS_END = WS_C + 8 * 18 * 36 * 128;

constexpr int L_WG = 0, L_WU = 36864, L_WDM = 73728;
constexpr int L_XN = 110592, XN_STR = 1168, L_WQ = 119936;
constexpr int L_A2 = 119936  , A2_STR = 272, L_HB = 129792, L_OB = 131072, OB_STR = 528;
constexpr int L_SP = 138368, SP_STR = 1088, L_Q = 140544, L_OP = 141056, L_ST = 145152  , L_FL = 145408, L_END = 145536;
enum { C_XN = 0, C_Q, C_M, C_P, C_O, C_VI, C_WO, C_PUB, C_N };

constexpr int AUX_NT = 2, AUX_SC1 = 16, RSRC3 = 0x00020000;
constexpr u32 O_GATE = 1270;
constexpr u32 MLP_GATE = 1200;
constexpr u32 O_DELAY = 30, A_DELAY = 40, G_DELAY = 50, C_DELAY = 15;
constexpr u32 CODE_BYTES = 22528;
#define DELAY(n) do { const u64 tq_ = now(); while (now() - tq_ < (n)) { } } while (0)
constexpr u64 SPIN_TICKS = 2000000ull;

struct Params {
  const u16 *x, *kc, *vc, *wq, *wo, *wr, *wg, *wu, *wd;
  u16* out;
  unsigned char* ws;
};

__device__ __forceinline__ float as_f(u32 x) { return __builtin_bit_cast(float, x); }
__device__ __forceinline__ u32 as_u(float x) { return __builtin_bit_cast(u32, x); }
__device__ __forceinline__ float bf_lo(u32 x) { return as_f(x << 16); }
__device__ __forceinline__ float bf_hi(u32 x) { return as_f(x & 0xffff0000u); }
__device__ __forceinline__ u32 pk_bf16(float a, float b) { v2f f = {a, b}; return __builtin_bit_cast(u32, __builtin_convertvector(f, v2bf)); }
__device__ __forceinline__ float rbf(float a) { return bf_lo(pk_bf16(a, a)); }
struct Tag { u32 lo, hi, t0, t1, t2, t3; };
__device__ __forceinline__ Tag make_tag(u32 lo, u32 hi) {
  return Tag{lo, hi, lo & 0x3ffu, (lo >> 10) & 0x3ffu, (lo >> 20) & 0x3ffu, hi & 0x3ffu};
}
__device__ __forceinline__ v4u tag_f32(v4f v, const Tag& t) {
  const v4u u = __builtin_bit_cast(v4u, v);
  return v4u{(u.x & ~0x3ffu) | t.t0, (u.y & ~0x3ffu) | t.t1, (u.z & ~0x3ffu) | t.t2, (u.w & ~0x3ffu) | t.t3};
}
__device__ __forceinline__ bool ok_f32(v4u u, const Tag& t) { return (((u.x ^ t.t0) | (u.y ^ t.t1) | (u.z ^ t.t2) | (u.w ^ t.t3)) & 0x3ffu) == 0u; }
__device__ __forceinline__ u32 xo(u32 a, u32 b, u32 e) { return __builtin_amdgcn_bitop3_b32(a, b, e, 0xBE); }
__device__ __forceinline__ u32 bad_f32(v4u u, const Tag& t, u32 e) { return xo(u.w, t.t3, xo(u.z, t.t2, xo(u.y, t.t1, xo(u.x, t.t0, e)))); }
__device__ __forceinline__ bool all_ok(u32 e) { return __all((e & 0x3ffu) == 0u); }
__device__ __forceinline__ v4f val_f32(v4u u) { return __builtin_bit_cast(v4f, v4u{u.x & ~0x3ffu, u.y & ~0x3ffu, u.z & ~0x3ffu, u.w & ~0x3ffu}); }
template <int C> __device__ __forceinline__ float dpp(float x) {
  return as_f((u32)__builtin_amdgcn_mov_dpp((int)as_u(x), C, 0xf, 0xf, true));
}
__device__ __forceinline__ float sum16(float x) { x += dpp<0xB1>(x); x += dpp<0x4E>(x); x += dpp<0x141>(x); x += dpp<0x140>(x); return x; }
__device__ __forceinline__ float max16(float x) {
  x = fmaxf(x, dpp<0xB1>(x)); x = fmaxf(x, dpp<0x4E>(x)); x = fmaxf(x, dpp<0x141>(x)); x = fmaxf(x, dpp<0x140>(x)); return x;
}
__device__ __forceinline__ float add_x16(float x) { auto s = __builtin_amdgcn_permlane16_swap(as_u(x), as_u(x), false, false); return as_f(s[0]) + as_f(s[1]); }
__device__ __forceinline__ float add_x32(float x) { auto s = __builtin_amdgcn_permlane32_swap(as_u(x), as_u(x), false, false); return as_f(s[0]) + as_f(s[1]); }
__device__ __forceinline__ float max_x16(float x) { auto s = __builtin_amdgcn_permlane16_swap(as_u(x), as_u(x), false, false); return fmaxf(as_f(s[0]), as_f(s[1])); }
__device__ __forceinline__ float rows_sum_t(v4f v) {
  auto p = __builtin_amdgcn_permlane16_swap(as_u(v[0]), as_u(v[1]), false, false);
  auto q = __builtin_amdgcn_permlane16_swap(as_u(v[2]), as_u(v[3]), false, false);
  auto z = __builtin_amdgcn_permlane32_swap(as_u(as_f(p[0]) + as_f(p[1])), as_u(as_f(q[0]) + as_f(q[1])), false, false);
  return as_f(z[0]) + as_f(z[1]);
}
__device__ __forceinline__ float sum32(float x) { return add_x16(sum16(x)); }
__device__ __forceinline__ float sum64(float x) { return add_x32(add_x16(sum16(x))); }

__device__ __forceinline__ v4f mfma(v4u a, v4u b, v4f c) {
  return __builtin_amdgcn_mfma_f32_16x16x32_bf16(__builtin_bit_cast(v8bf, a), __builtin_bit_cast(v8bf, b), c, 0, 0, 0);
}
__device__ __forceinline__ v4u lds128(lds_t L, int o) { return *(const LAS v4u*)(L + o); }
__device__ __forceinline__ v2u lds64(lds_t L, int o) { return *(const LAS v2u*)(L + o); }
__device__ __forceinline__ void sts64(lds_t L, int o, v2u v) { *(LAS v2u*)(L + o) = v; }
__device__ __forceinline__ v2u ldstr(lds_t L, int o) {
  return __builtin_bit_cast(v2u, __builtin_amdgcn_ds_read_tr16_b64_v4i16((LAS v4s*)(L + o)));
}
__device__ __forceinline__ v4u cat(v2u a, v2u b) { return v4u{a.x, a.y, b.x, b.y}; }
__device__ __forceinline__ u64 now() { return __builtin_amdgcn_s_memrealtime(); }

__device__ __forceinline__ void dma16(u32 ldsaddr, const void* gbase, u32 voff) {
  u32 keep;
  asm volatile("s_nop 4\n\ts_mov_b32 %0, m0\n\ts_mov_b32 m0, %2\n\ts_nop 0\n\tglobal_load_lds_dwordx4 %1, %3 nt\n\ts_mov_b32 m0, %0"
               : "=&s"(keep) : "v"(voff), "s"(ldsaddr), "s"(gbase) : "memory");
}
#define VMCNT_(n) asm volatile("s_waitcnt vmcnt(" #n ")" ::: "memory")
#define VMCNT(n) VMCNT_(n)
#define LGKM0() asm volatile("s_waitcnt lgkmcnt(0)" ::: "memory")
#define FENCE() asm volatile("" ::: "memory")

__device__ __forceinline__ void sig(lds_t L, int c, int lane) {
  LGKM0();
  if (lane == 0) __hip_atomic_fetch_add((LAS u32*)(L + L_FL) + c, 1u, __ATOMIC_RELAXED, __HIP_MEMORY_SCOPE_WORKGROUP);
  FENCE();
}
__device__ __forceinline__ void waitc(lds_t L, int c, u32 n) {
  volatile LAS u32* p = (volatile LAS u32*)(L + L_FL) + c;
  while ((u32)__builtin_amdgcn_readfirstlane(*p) < n) __builtin_amdgcn_s_sleep(1);
  FENCE();
}
__device__ __forceinline__ void waitc_fast(lds_t L, int c, u32 n) {
  volatile LAS u32* p = (volatile LAS u32*)(L + L_FL) + c;
  while ((u32)__builtin_amdgcn_readfirstlane(*p) < n) { }
  FENCE();
}
__device__ __forceinline__ v4u ld_sc1(__amdgpu_buffer_rsrc_t rs, int off) { FENCE(); return __builtin_amdgcn_raw_buffer_load_b128(rs, off, 0, AUX_SC1); }
__device__ __forceinline__ void st_sc1(__amdgpu_buffer_rsrc_t rs, int off, v4u v) { __builtin_amdgcn_raw_buffer_store_b128(v, rs, off, 0, AUX_SC1); }

__device__ __forceinline__ void transpose8(v4u (&X)[8]) {
  v4u Fo[8];
#pragma unroll
  for (int t = 0; t < 4; ++t)
#pragma unroll
    for (int m = 0; m < 4; ++m) {
      Fo[2 * t][m] = __builtin_amdgcn_perm(X[2 * m + 1][t], X[2 * m][t], 0x05040100u);
      Fo[2 * t + 1][m] = __builtin_amdgcn_perm(X[2 * m + 1][t], X[2 * m][t], 0x07060302u);
    }
#pragma unroll
  for (int e = 0; e < 8; ++e) X[e] = Fo[e];
}

#define PIN8(X) do { _Pragma("unroll") for (int e_ = 0; e_ < 8; ++e_) asm volatile("" : "+v"(X[e_])); } while (0)
__device__ __forceinline__ float ln_rden(float var) { return __builtin_amdgcn_rcpf(rbf(__builtin_amdgcn_sqrtf(rbf(rbf(var) + 1e-5f)))); }
__device__ __forceinline__ float fexp(float x) { return __builtin_amdgcn_exp2f(x * 1.4426950408889634f); }
#define BARRIER() do { LGKM0(); __builtin_amdgcn_s_barrier(); FENCE(); } while (0)


__device__ __forceinline__ v2f unpk(u32 x) { return v2f{bf_lo(x), bf_hi(x)}; }
__device__ __forceinline__ u32 pk2(v2f f) { return __builtin_bit_cast(u32, __builtin_convertvector(f, v2bf)); }
__device__ __forceinline__ Tag tag_of_epoch(v2u ep) {
  const u64 tag = (((u64)ep.y << 32) | ep.x) * 0x9E3779B97F4A7C15ull + 0x632BE59BD9B4E019ull;
  return make_tag((u32)tag, (u32)(tag >> 32));
}
__device__ __forceinline__ v2u load_epoch(__amdgpu_buffer_rsrc_t rs) {
  int zo = WS_EPOCH;
  asm volatile("" : "+v"(zo));
  return __builtin_amdgcn_raw_buffer_load_b64(rs, zo, 0, AUX_SC1);
}
__device__ __forceinline__ v4f mfma16(v2u a, v2u b, v4f c) {
  return __builtin_amdgcn_mfma_f32_16x16x16bf16_1k(__builtin_bit_cast(v4s, a), __builtin_bit_cast(v4s, b), c, 0, 0, 0);
}
#define PINV(x) asm volatile("" : "+v"(x))
#define EXPIRED() (now() - t_start > SPIN_TICKS)

__global__ void __launch_bounds__(512, 2) fused_decode(Params p) {
  __shared__ __attribute__((aligned(16))) unsigned char smem[L_END];
  lds_t L = (lds_t)smem;
  u64 kstart;
  asm volatile("s_getpc_b64 %0" : "=s"(kstart));
  kstart &= ~255ull;
  const u64 t_start = now();
  const int tid = threadIdx.x, lane = tid & 63, w = __builtin_amdgcn_readfirstlane(tid >> 6);
  const int c = blockIdx.x, x = c & 7, idx = c >> 3;
  const int hkv = 2 * x + (idx >> 4), j16 = idx & 15, b = j16 >> 1, half = j16 & 1;
  const int pair = b * 16 + hkv;
  const bool is_mlp = idx < 18, is_wo = idx >= 18 && idx < 30, is_red = idx >= 18 && idx < 27;
  const int pm = 18 * x + idx;
  const int hkv2 = 2 * x + (idx - 18) / 6, ng = (idx - 18) % 6;
  const int gp = idx - 18;
  const int r = lane >> 4, cc = lane & 15, q4 = cc >> 2, pp = cc & 3;
  const u32 lbase = (u32)(uintptr_t)L;
  const int rot = ((idx >> 1) + x) & 3;
  const __amdgpu_buffer_rsrc_t rs = __builtin_amdgcn_make_buffer_rsrc(p.ws, 0, WS_END, RSRC3);
  Tag tg;

  if (w < 4) {
    const int k = w;
    {
      v4u Kr[16][2];
      const __amdgpu_buffer_rsrc_t rk = __builtin_amdgcn_make_buffer_rsrc(
          (void*)(p.kc + ((size_t)pair * SK + (SK - WIN) + half * 512 + 128 * k) * DH), 0, 128 * 256, RSRC3);
      const int voffk = (cc & 7) * 256 + (cc >> 3) * 64 + r * 16;
#pragma unroll
      for (int n = 0; n < 32; ++n) {
        Kr[n >> 1][n & 1] = __builtin_amdgcn_raw_buffer_load_b128(rk, voffk, ((((n >> 3) + rot) & 3) * 4 + ((n >> 1) & 3)) * 2048 + (n & 1) * 128, AUX_NT);
        if (n == 0) __builtin_amdgcn_s_barrier();
        __builtin_amdgcn_sched_barrier(0);
      }
      const v2u ep = load_epoch(rs);
      const int qoff = WS_Q + ((hkv * 8 + b) * 4 + k) * 2048 + lane * 16;
      v4u g = ld_sc1(rs, qoff), g2 = ld_sc1(rs, qoff + 1024);
      u32 msk[4];
#pragma unroll
      for (int b4 = 0; b4 < 4; ++b4) { msk[b4] = (((cc >> 1) & 3) == b4) ? 0xffffffffu : 0u; PINV(msk[b4]); }
      int qa = L_Q + ((cc & 1) * 128 + 32 * (cc >> 3) + 8 * r) * 2;
      int sa = L_SP + (cc & 1) * SP_STR + (128 * k + 8 * ((cc >> 1) & 3) + 4 * r) * 2;
      PINV(qa); PINV(sa);
      tg = tag_of_epoch(ep);
      while (!(__all(g.z == tg.lo && g.w == tg.hi && g2.z == tg.lo && g2.w == tg.hi) || EXPIRED())) { g = ld_sc1(rs, qoff); g2 = ld_sc1(rs, qoff + 1024); }
      {
        v4f qv = {as_f(g.x), as_f(g.y), as_f(g2.x), as_f(g2.y)};
        const float qs = rows_sum_t(qv);
        *(LAS u16*)(L + L_Q + (64 * k + 4 * cc + r) * 2) = (u16)(pk_bf16(qs, qs) & 0xffffu);
      }
      sig(L, C_Q, lane);
      waitc_fast(L, C_Q, 4);
      v4u Bm[4][2];
#pragma unroll
      for (int s = 0; s < 2; ++s) {
        const v4u q = lds128(L, qa + 128 * s);
#pragma unroll
        for (int b4 = 0; b4 < 4; ++b4) Bm[b4][s] = q & msk[b4];
      }
      v4f Dg[4];
#pragma unroll
      for (int g4 = 0; g4 < 4; ++g4) {
        Dg[g4] = v4f{0.f, 0.f, 0.f, 0.f};
#pragma unroll
        for (int b4 = 0; b4 < 4; ++b4)
#pragma unroll
          for (int s = 0; s < 2; ++s) Dg[g4] = mfma(Kr[4 * g4 + b4][s], Bm[b4][s], Dg[g4]);
      }
#pragma unroll
      for (int g4 = 0; g4 < 4; ++g4) {
        float sc[4];
#pragma unroll
        for (int t = 0; t < 4; ++t) {
          auto sw = __builtin_amdgcn_permlane32_swap(as_u(Dg[g4][t]), as_u(Dg[g4][t]), false, false);
          sc[t] = as_f(sw[0]) + dpp<0x128>(as_f(sw[1]));
        }
        const float SC = 0.08838834764831845f;
        const v2u o = {pk2(unpk(pk_bf16(sc[0], sc[1])) * SC), pk2(unpk(pk_bf16(sc[2], sc[3])) * SC)};
        if (lane < 32 && cc < 8) sts64(L, sa + 64 * ((g4 + rot) & 3), o);
      }
    }
    LGKM0();
    {
      const int hd = lane >> 5, so = L_SP + hd * SP_STR + (128 * k + 4 * (lane & 31)) * 2;
      const v2u sv = lds64(L, so);
      const v2f f0 = unpk(sv.x), f1 = unpk(sv.y);
      const float mw = max_x16(max16(fmaxf(fmaxf(f0.x, f0.y), fmaxf(f1.x, f1.y))));
      if ((lane & 31) == 0) *(LAS float*)(L + L_ST + hd * 16 + k * 4) = mw;
      sig(L, C_M, lane);
      waitc_fast(L, C_M, 4);
      const v4f m4 = *(const LAS v4f*)(L + L_ST + hd * 16);
      const float m = fmaxf(fmaxf(m4[0], m4[1]), fmaxf(m4[2], m4[3]));
      const float L2E = 1.4426950408889634f;
      const v2f t0 = (f0 - m) * L2E, t1 = (f1 - m) * L2E;
      const v2f e0 = {__builtin_amdgcn_exp2f(t0.x), __builtin_amdgcn_exp2f(t0.y)}, e1 = {__builtin_amdgcn_exp2f(t1.x), __builtin_amdgcn_exp2f(t1.y)};
      sts64(L, so, v2u{pk2(e0), pk2(e1)});
      const float lw = sum32((e0.x + e0.y) + (e1.x + e1.y));
      if ((lane & 31) == 0) { *(LAS float*)(L + L_ST + 32 + hd * 16 + k * 4) = lw; if (k == 0) *(LAS float*)(L + L_ST + 64 + hd * 4) = m; }
      sig(L, C_P, lane);
    }
    if (k == 0) {
      waitc(L, C_P, 4);
      const v4f l0 = *(const LAS v4f*)(L + L_ST + 32), l1 = *(const LAS v4f*)(L + L_ST + 48);
      const v2f mm = *(const LAS v2f*)(L + L_ST + 64);
      if (lane == 0) st_sc1(rs, WS_M + ((hkv * 8 + b) * 2 + half) * 16, tag_f32(v4f{mm.x, (l0[0] + l0[1]) + (l0[2] + l0[3]), mm.y, (l1[0] + l1[1]) + (l1[2] + l1[3])}, tg));
    }
    if (is_mlp) {
      waitc(L, C_PUB, 1);
      while (now() - t_start < MLP_GATE) __builtin_amdgcn_s_sleep(2);
    }
    if (is_wo) waitc(L, C_VI, 4);
    if (is_mlp) {
      const size_t go = (size_t)(144 * (pm & 3)) * 9216 + (size_t)(pm >> 2) * 256;
      const unsigned char* wgb = (const unsigned char*)p.wg + go;
      const unsigned char* wub = (const unsigned char*)p.wu + go;
      const u32 vg = (u32)(r * 9216 + ((cc ^ (2 * (r + 4 * ((k >> 1) & 1)))) * 16));
#pragma unroll
      for (int i = 0; i < 18; ++i) {
        const int ii = k + 4 * (i >> 1);
        dma16(lbase + ((i & 1) ? L_WU : L_WG) + ii * 1024, ((i & 1) ? wub : wgb) + (size_t)ii * (4 * 9216), vg);
      }
      const unsigned char* wdb = (const unsigned char*)p.wd + (size_t)(128 * (pm >> 2)) * 1152 + (pm & 3) * 288;
#pragma unroll
      for (int i = 0; i < 9; ++i) {
        const int g = 64 * (k + 4 * i) + lane, row = g / 18;
        dma16(lbase + L_WDM + (k + 4 * i) * 1024, wdb, (u32)(row * 1152 + (g - 18 * row) * 16));
      }
      VMCNT(9);
      sig(L, C_WO, lane);
    } else if (is_wo) {
      const unsigned char* wob = (const unsigned char*)p.wo + (size_t)(hkv2 * 256) * 1152 + ng * 192;
#pragma unroll
      for (int i = 0; i < 12; ++i) {
        const int ii = k + 4 * i, g = 64 * ii + lane, row = g / 12, ph = g - 12 * row;
        dma16(lbase + L_WG + ii * 1024, wob, (u32)(row * 1152 + ((ph ^ (2 * ((row >> 2) & 1))) * 16)));
      }
    } else {
      return;
    }
  } else {
    const int v = w - 4;
    __builtin_amdgcn_s_setprio(3);
    const int l32 = lane & 31, bq = 2 * v + (lane >> 5), cg = j16 & 3, rg = j16 >> 2;
    v4u codev = {0u, 0u, 0u, 0u};
    if (v == 3) {
      const u64 base = kstart;
      const u32 o = (u32)idx * 1024u + (u32)lane * 16u;
      if (o + 16u <= CODE_BYTES) asm volatile("global_load_dwordx4 %0, %1, %2" : "=v"(codev) : "v"(o), "s"(base) : "memory");
    }
    u32 xr[9];
    {
      const u16* xp = p.x + bq * D + 2 * l32;
      asm volatile("global_load_dword %0, %9, off\n\tglobal_load_dword %1, %9, off offset:128\n\tglobal_load_dword %2, %9, off offset:256\n\t"
                   "global_load_dword %3, %9, off offset:384\n\tglobal_load_dword %4, %9, off offset:512\n\tglobal_load_dword %5, %9, off offset:640\n\t"
                   "global_load_dword %6, %9, off offset:768\n\tglobal_load_dword %7, %9, off offset:896\n\tglobal_load_dword %8, %9, off offset:1024"
                   : "=&v"(xr[0]), "=&v"(xr[1]), "=&v"(xr[2]), "=&v"(xr[3]), "=&v"(xr[4]), "=&v"(xr[5]), "=&v"(xr[6]), "=&v"(xr[7]), "=&v"(xr[8])
                   : "v"(xp) : "memory");
    }
    const v2u ep = load_epoch(rs);
    {
      const unsigned char* wqb = (const unsigned char*)p.wq + (size_t)(144 * rg) * 8192 + (hkv * 256 + 64 * cg) * 2;
      const int rr = lane >> 3;
      const u32 vo = (u32)(rr * 8192 + (((lane & 7) ^ (2 * (rr >> 1))) * 16));
#pragma unroll
      for (int i = 0; i < 5; ++i) {
        const int ii = v + 4 * i;
        if (i < 4 || v < 2) dma16(lbase + L_WQ + ii * 1024, wqb + (size_t)ii * (8 * 8192), vo);
      }
    }
    if (tid - 256 < C_N) ((LAS u32*)(L + L_FL))[tid - 256] = 0u;
    LGKM0();
    __builtin_amdgcn_s_barrier();
    FENCE();
    {
      asm volatile("s_waitcnt vmcnt(5)" : "+v"(xr[0]), "+v"(xr[1]), "+v"(xr[2]), "+v"(xr[3]), "+v"(xr[4]), "+v"(xr[5]), "+v"(xr[6]), "+v"(xr[7]), "+v"(xr[8]));
      v2f f[9];
#pragma unroll
      for (int i = 0; i < 9; ++i) f[i] = unpk(xr[i]);
      v2f s = f[0];
#pragma unroll
      for (int i = 1; i < 9; ++i) s += f[i];
      const float mean = sum32(s.x + s.y) * (1.0f / D);
      v2f s2 = {0.f, 0.f};
#pragma unroll
      for (int i = 0; i < 9; ++i) { const v2f d = f[i] - mean; s2 += d * d; }
      const float rd = ln_rden(sum32(s2.x + s2.y) * (1.0f / D)), m = rbf(mean);
#pragma unroll
      for (int i = 0; i < 9; ++i) *(LAS u32*)(L + L_XN + bq * XN_STR + (l32 + 32 * i) * 4) = pk2(unpk(pk2(f[i] - m)) * rd);
    }
    tg = tag_of_epoch(ep);
    VMCNT(0);
    asm volatile("" :: "v"(codev));
    sig(L, C_XN, lane);
    {
      const int z = 4 * (r & 1) + q4;
      const int aoff = L_WQ + (4 * r + q4) * 128 + (((2 * v + (pp >> 1)) ^ (2 * (z >> 1))) * 16) + (pp & 1) * 8;
      const int boff = L_XN + (cc & 7) * XN_STR + (144 * rg + 4 * r) * 2;
      const int qo = WS_Q + ((hkv * 8 + cc) * 4 + cg) * 2048 + rg * 256 + (4 * v + r) * 16;
      waitc_fast(L, C_XN, 4);
      v4f Dq = mfma16(ldstr(L, aoff + 128 * 128), lds64(L, boff + 256), v4f{0.f, 0.f, 0.f, 0.f});
#pragma unroll
      for (int sg = 0; sg < 4; ++sg)
        Dq = mfma(cat(ldstr(L, aoff + sg * 4096), ldstr(L, aoff + sg * 4096 + 2048)), cat(lds64(L, boff + sg * 64), lds64(L, boff + sg * 64 + 32)), Dq);
      if (cc < 8) {
        st_sc1(rs, qo, v4u{as_u(Dq[0]), as_u(Dq[1]), tg.lo, tg.hi});
        st_sc1(rs, qo + 1024, v4u{as_u(Dq[2]), as_u(Dq[3]), tg.lo, tg.hi});
      }
    }
    __builtin_amdgcn_s_setprio(0);
    v4u Vr[4][8];
    {
      const __amdgpu_buffer_rsrc_t rv = __builtin_amdgcn_make_buffer_rsrc(
          (void*)(p.vc + ((size_t)pair * SK + (SK - WIN) + half * 512 + 128 * v) * DH), 0, 128 * 256, RSRC3);
      const int voffv = 8 * r * 256 + cc * 16;
#pragma unroll
      for (int n = 0; n < 32; ++n) {
        Vr[n >> 3][n & 7] = __builtin_amdgcn_raw_buffer_load_b128(rv, voffv, (32 * (((n >> 3) + rot) & 3) + (n & 7)) * 256, AUX_NT);
        __builtin_amdgcn_sched_barrier(0);
      }
    }
    sig(L, C_VI, lane);
    transpose8(Vr[0]); PIN8(Vr[0]);
    transpose8(Vr[1]); PIN8(Vr[1]);
    transpose8(Vr[2]); PIN8(Vr[2]);
    waitc(L, C_P, 4);
    v4f acc[8];
#pragma unroll
    for (int e = 0; e < 8; ++e) acc[e] = v4f{0.f, 0.f, 0.f, 0.f};
    const int poff = L_SP + ((cc >> 2) & 1) * SP_STR + (128 * v + 8 * r) * 2;
#pragma unroll
    for (int bb = 0; bb < 3; ++bb) {
      const v4u A = lds128(L, poff + ((bb + rot) & 3) * 64);
#pragma unroll
      for (int e = 0; e < 8; ++e) acc[e] = mfma(A, Vr[bb][e], acc[e]);
    }
    v4u A3 = lds128(L, poff + ((3 + rot) & 3) * 64);
    int opa = L_OP + v * 1024 + (r & 1) * 512 + cc * 32;
    int ooff = WS_O + ((hkv * 8 + b) * 2 + half) * 1024 + lane * 16;
    PINV(opa); PINV(ooff); PINV(A3);
#pragma unroll
    for (int e = 0; e < 8; ++e) PINV(acc[e]);
    __builtin_amdgcn_s_setprio(3);
    __builtin_amdgcn_sched_barrier(0);
    transpose8(Vr[3]);
#pragma unroll
    for (int e = 0; e < 8; ++e) acc[e] = mfma(A3, Vr[3][e], acc[e]);
    if (r < 2) {
#pragma unroll
      for (int e = 0; e < 8; ++e) *(LAS float*)(L + opa + 4 * e) = acc[e][0];
    }
    sig(L, C_O, lane);
    if (v == 0) {
      waitc_fast(L, C_O, 4);
      const v4f o = (*(const LAS v4f*)(L + L_OP + lane * 16) + *(const LAS v4f*)(L + L_OP + 1024 + lane * 16)) +
                    (*(const LAS v4f*)(L + L_OP + 2048 + lane * 16) + *(const LAS v4f*)(L + L_OP + 3072 + lane * 16));
      st_sc1(rs, ooff, tag_f32(o, tg));
      sig(L, C_PUB, lane);
    }
    if (!is_mlp && !is_wo) return;
  }

  if (is_wo) {
    int oa = WS_O + ((hkv2 * 8 + w) * 2) * 1024 + lane * 16;
    const int col = 96 * ng + 16 * w + 4 * r;
    int ho = WS_A + ((cc * 9 + (col >> 6)) * 16 + hkv2) * 256 + (col & 63) * 4;
    int ob = L_OB + (cc & 7) * OB_STR + r * 8;
    v4u Wf[8];
    v2u xv = {0u, 0u};
    if (w == 0 && is_red) xv = *(const GAS v2u*)(p.x + x * D + 64 * gp + 4 * cc);
    const int bt = (w & 3) + 4 * ((lane >> 1) & 1), ma = WS_M + ((hkv2 * 8 + bt) * 2 + (lane & 1)) * 16;
    v4u gm = {0u, 0u, 0u, 0u};
    if (w < 4) gm = ld_sc1(rs, ma);
    PINV(oa); PINV(ho); PINV(ob);
    waitc(L, C_PUB, 1);
    DELAY(O_DELAY);
    while (now() - t_start < O_GATE) { }
    v4u ga = ld_sc1(rs, oa), gb = ld_sc1(rs, oa + 1024);
    if (w < 4) {
      while (!(__all(ok_f32(gm, tg)) || EXPIRED())) gm = ld_sc1(rs, ma);
      const v4f st = val_f32(gm);
      float cf[2];
#pragma unroll
      for (int hd = 0; hd < 2; ++hd) {
        const float m_own = st[2 * hd], l_own = st[2 * hd + 1], m_oth = dpp<0xB1>(m_own), l_oth = dpp<0xB1>(l_own);
        const float M = fmaxf(m_own, m_oth), w_own = fexp(m_own - M), w_oth = fexp(m_oth - M);
        cf[hd] = w_own * __builtin_amdgcn_rcpf((lane & 1) ? w_oth * l_oth + w_own * l_own : w_own * l_own + w_oth * l_oth);
      }
      if (lane < 4) { *(LAS float*)(L + L_ST + 128 + bt * 16 + (lane & 1) * 4) = cf[0]; *(LAS float*)(L + L_ST + 128 + bt * 16 + 8 + (lane & 1) * 4) = cf[1]; }
      sig(L, C_WO, lane);
    }
    waitc(L, C_WO, 4);
    if (w < 6) {
      const int a = L_WG + (4 * r + q4) * 192 + ((2 * (w ^ (r & 1)) + (pp >> 1)) * 16) + (pp & 1) * 8;
#pragma unroll
      for (int sg = 0; sg < 8; ++sg) { Wf[sg] = cat(ldstr(L, a + sg * 6144), ldstr(L, a + sg * 6144 + 3072)); PINV(Wf[sg]); }
    }
    v2f co = *(const LAS v2f*)(L + L_ST + 128 + w * 16 + (lane >> 5) * 8);
    PINV(co);
    while (!(all_ok(bad_f32(gb, tg, bad_f32(ga, tg, 0u))) || EXPIRED())) {
      if (!all_ok(bad_f32(ga, tg, 0u))) ga = ld_sc1(rs, oa);
      if (!all_ok(bad_f32(gb, tg, 0u))) gb = ld_sc1(rs, oa + 1024);
    }
    {
      const v4f o = val_f32(ga) * co.x + val_f32(gb) * co.y;
      sts64(L, L_OB + w * OB_STR + lane * 8, v2u{pk_bf16(o[0], o[1]), pk_bf16(o[2], o[3])});
    }
    BARRIER();
    if (w < 6) {
      v4u Bo[8];
#pragma unroll
      for (int sg = 0; sg < 8; ++sg) Bo[sg] = cat(lds64(L, ob + sg * 64), lds64(L, ob + sg * 64 + 32));
      asm volatile("" : "+v"(Bo[0]), "+v"(Bo[1]), "+v"(Bo[2]), "+v"(Bo[3]), "+v"(Bo[4]), "+v"(Bo[5]), "+v"(Bo[6]), "+v"(Bo[7]));
      v4f D0 = {0.f, 0.f, 0.f, 0.f}, D1 = {0.f, 0.f, 0.f, 0.f};
#pragma unroll
      for (int sg = 0; sg < 8; sg += 2) { D0 = mfma(Wf[sg], Bo[sg], D0); D1 = mfma(Wf[sg + 1], Bo[sg + 1], D1); }
      if (cc < 8) st_sc1(rs, ho, tag_f32(D0 + D1, tg));
    }
    if (is_red && w == 0) {
      const int off = WS_A + ((x * 9 + gp) * 16 + r) * 256 + cc * 16;
      const int so = WS_Bh + r * 18432 + x * 2304 + gp * 256 + cc * 16;
      const v2f x0 = unpk(xv.x), x1 = unpk(xv.y);
      DELAY(A_DELAY);
      v4u a0 = ld_sc1(rs, off), a1 = ld_sc1(rs, off + 1024), a2 = ld_sc1(rs, off + 2048), a3 = ld_sc1(rs, off + 3072);
      while (!(all_ok(bad_f32(a3, tg, bad_f32(a2, tg, bad_f32(a1, tg, bad_f32(a0, tg, 0u))))) || EXPIRED())) {
        if (!all_ok(bad_f32(a0, tg, 0u))) a0 = ld_sc1(rs, off);
        if (!all_ok(bad_f32(a1, tg, 0u))) a1 = ld_sc1(rs, off + 1024);
        if (!all_ok(bad_f32(a2, tg, 0u))) a2 = ld_sc1(rs, off + 2048);
        if (!all_ok(bad_f32(a3, tg, 0u))) a3 = ld_sc1(rs, off + 3072);
      }
      v4f t = (val_f32(a0) + val_f32(a1)) + (val_f32(a2) + val_f32(a3));
#pragma unroll
      for (int i = 0; i < 4; ++i) t[i] = add_x32(add_x16(t[i]));
      const u32 h01 = pk2(x0 + unpk(pk_bf16(t[0], t[1]))), h23 = pk2(x1 + unpk(pk_bf16(t[2], t[3])));
      const v4u h = {(h01 << 16) | (tg.lo & 0xffffu), (h01 & 0xffff0000u) | (tg.lo >> 16), (h23 << 16) | (tg.hi & 0xffffu), (h23 & 0xffff0000u) | (tg.hi >> 16)};
      st_sc1(rs, so, h);
      st_sc1(rs, so + 4 * 18432, h);
    }
    return;
  }

  v2u wr0 = {0u, 0u}, wr1 = {0u, 0u};
  u32 wr2 = 0u;
  if (w == x) { wr0 = *(const GAS v2u*)(p.wr + 4 * lane); wr1 = *(const GAS v2u*)(p.wr + 256 + 4 * lane); wr2 = *(const GAS u16*)(p.wr + 512 + lane); }
  v4u Gf[4], Uf[4], Df[2][4];
  v2u Gr, Ur;
  {
    waitc(L, C_WO, 4);
    const int a = (8 * r + q4) * 256 + (2 * (w ^ (q4 + 4 * (r & 1))) + (pp >> 1)) * 16 + (pp & 1) * 8;
#pragma unroll
    for (int sg = 0; sg < 4; ++sg) {
      Gf[sg] = cat(ldstr(L, L_WG + a + sg * 8192), ldstr(L, L_WG + a + sg * 8192 + 1024)); PINV(Gf[sg]);
      Uf[sg] = cat(ldstr(L, L_WU + a + sg * 8192), ldstr(L, L_WU + a + sg * 8192 + 1024)); PINV(Uf[sg]);
    }
    const int ar = (128 + 4 * r + q4) * 256 + (2 * (w ^ (q4 + 4 * ((r >> 1) & 1))) + (pp >> 1)) * 16 + (pp & 1) * 8;
    Gr = ldstr(L, L_WG + ar); Ur = ldstr(L, L_WU + ar); PINV(Gr); PINV(Ur);
  }
  int gso = WS_G + (pm * 8 + w) * 1024 + cc * 64 + r * 16;
  int ggo = WS_G + ((pm & ~3) * 8 + ((lane >> 2) & 7)) * 1024 + ((lane >> 5) * 8 + w) * 64 + (lane & 3) * 16;
  int xna = L_XN + w * XN_STR + lane * 8, xnb = L_XN + w * XN_STR + 1024 + lane * 2, boa = L_XN + (cc & 7) * XN_STR + (144 * (pm & 3)) * 2 + 16 * r,
      bob = L_XN + (cc & 7) * XN_STR + (144 * (pm & 3) + 128 + 4 * r) * 2, dfa = L_WDM + (4 * r + q4) * 288 + pp * 8 + w * 32;
  PINV(gso); PINV(ggo); PINV(xna); PINV(xnb); PINV(boa); PINV(bob); PINV(dfa);
  u32 n[5];
  v4u h0, h1;
  u32 h2;
  {
    const int off = WS_Bh + x * 18432 + w * 2304;
    const u32 t3 = (lane & 1) ? ((lane & 2) ? tg.hi >> 16 : tg.lo >> 16) : ((lane & 2) ? tg.hi & 0xffffu : tg.lo & 0xffffu);
    while (true) {
      h0 = ld_sc1(rs, off + lane * 16);
      h1 = ld_sc1(rs, off + 1024 + lane * 16);
      h2 = __builtin_amdgcn_raw_buffer_load_b32(rs, off + 2048 + lane * 4, 0, AUX_SC1);
      const u32 tl1 = tg.lo >> 16, th1 = tg.hi >> 16;
      const u32 e = xo(h2, t3, xo(h1.w, th1, xo(h1.z, tg.hi, xo(h1.y, tl1, xo(h1.x, tg.lo, xo(h0.w, th1, xo(h0.z, tg.hi, xo(h0.y, tl1, xo(h0.x, tg.lo, 0u)))))))));
      if (__all((e & 0xffffu) == 0u) || EXPIRED()) break;
      __builtin_amdgcn_s_sleep(1);
    }
    v2f f[4];
    f[0] = v2f{bf_hi(h0.x), bf_hi(h0.y)}; f[1] = v2f{bf_hi(h0.z), bf_hi(h0.w)};
    f[2] = v2f{bf_hi(h1.x), bf_hi(h1.y)}; f[3] = v2f{bf_hi(h1.z), bf_hi(h1.w)};
    const float f8 = bf_hi(h2);
    const float pv = as_f(__builtin_amdgcn_readfirstlane(as_u(f8)));
    const v2f d0 = f[0] - pv, d1 = f[1] - pv, d2 = f[2] - pv, d3 = f[3] - pv;
    const float d8 = f8 - pv;
    const v2f sv = (d0 + d1) + (d2 + d3), sq = (d0 * d0 + d1 * d1) + (d2 * d2 + d3 * d3);
    float s1 = sv.x + sv.y + d8, s2 = sq.x + sq.y + d8 * d8;
    s1 += dpp<0xB1>(s1); s2 += dpp<0xB1>(s2);
    s1 += dpp<0x4E>(s1); s2 += dpp<0x4E>(s2);
    s1 += dpp<0x141>(s1); s2 += dpp<0x141>(s2);
    s1 += dpp<0x140>(s1); s2 += dpp<0x140>(s2);
    { auto p = __builtin_amdgcn_permlane16_swap(as_u(s1), as_u(s2), false, false);
      const float t = as_f(p[0]) + as_f(p[1]);
      auto z = __builtin_amdgcn_permlane32_swap(as_u(t), as_u(t), false, false);
      const float tt = as_f(z[0]) + as_f(z[1]);
      auto y = __builtin_amdgcn_permlane16_swap(as_u(tt), as_u(tt), false, false);
      s1 = as_f(y[0]); s2 = as_f(y[1]); }
    const float dm = s1 * (1.0f / D), mean = pv + dm;
    const float rd = ln_rden(fmaxf(s2 * (1.0f / D) - dm * dm, 0.f)), m = rbf(mean);
#pragma unroll
    for (int i = 0; i < 4; ++i) n[i] = pk2(unpk(pk2(f[i] - m)) * rd);
    n[4] = pk_bf16(rbf(f8 - m) * rd, 0.f);
    sts64(L, xna, v2u{n[0], n[1]});
    sts64(L, xna + 512, v2u{n[2], n[3]});
    *(LAS u16*)(L + xnb) = (u16)n[4];
  }
  BARRIER();
  {
    v2u br = lds64(L, bob);
    v4u bx[4];
#pragma unroll
    for (int sg = 0; sg < 4; ++sg) bx[sg] = lds128(L, boa + 64 * sg);
    asm volatile("" : "+v"(br), "+v"(bx[0]), "+v"(bx[1]), "+v"(bx[2]), "+v"(bx[3]));
    v4f g = mfma16(Gr, br, v4f{0.f, 0.f, 0.f, 0.f}), u = mfma16(Ur, br, v4f{0.f, 0.f, 0.f, 0.f});
#pragma unroll
    for (int sg = 0; sg < 4; ++sg) {
      g = mfma(Gf[sg], bx[sg], g);
      u = mfma(Uf[sg], bx[sg], u);
    }
    st_sc1(rs, gso, tag_f32((cc < 8) ? g : u, tg));
  }
  {
#pragma unroll
    for (int sg = 0; sg < 4; ++sg) Df[0][sg] = cat(ldstr(L, dfa + sg * 9216), ldstr(L, dfa + sg * 9216 + 4608));
    if (w == 7) {
#pragma unroll
      for (int sg = 0; sg < 4; ++sg) Df[1][sg] = cat(ldstr(L, dfa + 32 + sg * 9216), ldstr(L, dfa + 32 + sg * 9216 + 4608));
    } else {
#pragma unroll
      for (int sg = 0; sg < 4; ++sg) Df[1][sg] = v4u{0u, 0u, 0u, 0u};
    }
    asm volatile("" : "+v"(Df[0][0]), "+v"(Df[0][1]), "+v"(Df[0][2]), "+v"(Df[0][3]), "+v"(Df[1][0]), "+v"(Df[1][1]), "+v"(Df[1][2]), "+v"(Df[1][3]));
  }
  int yo0, yo1;
  { const int t0 = 9 * (pm & 3) + w, t1 = 9 * (pm & 3) + 8;
    yo0 = WS_C + (((pm >> 2) * 36 + t0) * 8 + cc) * 64 + r * 16;
    yo1 = WS_C + (((pm >> 2) * 36 + t1) * 8 + cc) * 64 + r * 16; }
  int ba = L_A2 + (cc & 7) * A2_STR + r * 8;
  PINV(yo0); PINV(yo1); PINV(ba);
  {
    DELAY(G_DELAY);
    v4u g0 = ld_sc1(rs, ggo), g1 = ld_sc1(rs, ggo + 8192), g2 = ld_sc1(rs, ggo + 16384), g3 = ld_sc1(rs, ggo + 24576);
    while (!(all_ok(bad_f32(g3, tg, bad_f32(g2, tg, bad_f32(g1, tg, bad_f32(g0, tg, 0u))))) || EXPIRED())) {
      if (!all_ok(bad_f32(g0, tg, 0u))) g0 = ld_sc1(rs, ggo);
      if (!all_ok(bad_f32(g1, tg, 0u))) g1 = ld_sc1(rs, ggo + 8192);
      if (!all_ok(bad_f32(g2, tg, 0u))) g2 = ld_sc1(rs, ggo + 16384);
      if (!all_ok(bad_f32(g3, tg, 0u))) g3 = ld_sc1(rs, ggo + 24576);
    }
    const v4f s4 = (val_f32(g0) + val_f32(g1)) + (val_f32(g2) + val_f32(g3));
    const u32 p01 = pk_bf16(s4[0], s4[1]), p23 = pk_bf16(s4[2], s4[3]);
    auto sa = __builtin_amdgcn_permlane32_swap(p01, p23, false, false);
    const v2f g = unpk(sa[0]), u = unpk(sa[1]);
    const v2f sl = {g.x * __builtin_amdgcn_rcpf(1.0f + fexp(-g.x)), g.y * __builtin_amdgcn_rcpf(1.0f + fexp(-g.y))};
    *(LAS u32*)(L + L_A2 + w * A2_STR + (4 * (lane & 31) + 2 * (lane >> 5)) * 2) = pk2(unpk(pk2(sl)) * u);
  }
  BARRIER();
  {
    v4u Ba[4];
#pragma unroll
    for (int sg = 0; sg < 4; ++sg) Ba[sg] = cat(lds64(L, ba + sg * 64), lds64(L, ba + sg * 64 + 32));
    asm volatile("" : "+v"(Ba[0]), "+v"(Ba[1]), "+v"(Ba[2]), "+v"(Ba[3]));
    v4f y0 = {0.f, 0.f, 0.f, 0.f}, y1 = {0.f, 0.f, 0.f, 0.f};
#pragma unroll
    for (int sg = 0; sg < 4; ++sg) y0 = mfma(Df[0][sg], Ba[sg], y0);
    if (cc < 8) st_sc1(rs, yo0, tag_f32(y0, tg));
    if (w == 7) {
#pragma unroll
      for (int sg = 0; sg < 4; ++sg) y1 = mfma(Df[1][sg], Ba[sg], y1);
      if (cc < 8) st_sc1(rs, yo1, tag_f32(y1, tg));
    }
  }
  if (w == x) {
    const int tq = lane >> 3, ch = lane & 7;
    const int off = WS_C + ((tq * 36 + 2 * idx + (ch >> 2)) * 8 + x) * 64 + (ch & 3) * 16;
    const int off4 = off + (32 - (tq & 4)) * (36 * 512);
    sts64(L, L_HB + lane * 8, v2u{(h0.x >> 16) | (h0.y & 0xffff0000u), (h0.z >> 16) | (h0.w & 0xffff0000u)});
    sts64(L, L_HB + 512 + lane * 8, v2u{(h1.x >> 16) | (h1.y & 0xffff0000u), (h1.z >> 16) | (h1.w & 0xffff0000u)});
    *(LAS u16*)(L + L_HB + 1024 + lane * 2) = (u16)(h2 >> 16);
    v2f rr = unpk(n[0]) * unpk(wr0.x) + unpk(n[1]) * unpk(wr0.y);
    rr += unpk(n[2]) * unpk(wr1.x) + unpk(n[3]) * unpk(wr1.y);
    const float rt = rbf(sum64(rr.x + rr.y + bf_lo(n[4]) * bf_lo(wr2)));
    float rsel = rbf(__builtin_amdgcn_rcpf(1.0f + fexp(-rt)));
    LGKM0();
    const int col = 32 * idx + 4 * (cc & 7) + r;
    float hn = bf_lo(*(const LAS u16*)(L + L_HB + col * 2)), xn = bf_lo(*(const LAS u16*)(L + L_XN + x * XN_STR + col * 2));
    PINV(rsel); PINV(hn); PINV(xn);
    DELAY(C_DELAY);
    v4u g0 = ld_sc1(rs, off), g1 = ld_sc1(rs, off + 147456), g2 = ld_sc1(rs, off + 2 * 147456), g3 = ld_sc1(rs, off + 3 * 147456), g4 = ld_sc1(rs, off4);
    while (!(all_ok(bad_f32(g4, tg, bad_f32(g3, tg, bad_f32(g2, tg, bad_f32(g1, tg, bad_f32(g0, tg, 0u)))))) || EXPIRED())) {
      if (!all_ok(bad_f32(g0, tg, 0u))) g0 = ld_sc1(rs, off);
      if (!all_ok(bad_f32(g1, tg, 0u))) g1 = ld_sc1(rs, off + 147456);
      if (!all_ok(bad_f32(g2, tg, 0u))) g2 = ld_sc1(rs, off + 2 * 147456);
      if (!all_ok(bad_f32(g3, tg, 0u))) g3 = ld_sc1(rs, off + 3 * 147456);
      if (!all_ok(bad_f32(g4, tg, 0u))) g4 = ld_sc1(rs, off4);
    }
    v4f s = (val_f32(g0) + val_f32(g1)) + (val_f32(g2) + val_f32(g3));
    if (tq < 4) s += val_f32(g4);
#pragma unroll
    for (int i = 0; i < 4; ++i) s[i] += dpp<0x128>(s[i]);
    const float y = rows_sum_t(s);
    const float o = hn + rbf(xn + rbf(rbf(y) * rsel));
    if (cc < 8) *(GAS u16*)(p.out + x * D + col) = (u16)(pk_bf16(o, o) & 0xffffu);
    if (c == 0 && lane == 0) {
      const u64 e = __hip_atomic_load((const GAS u64*)(p.ws + WS_EPOCH), __ATOMIC_RELAXED, __HIP_MEMORY_SCOPE_AGENT);
      __hip_atomic_store((GAS u64*)(p.ws + WS_EPOCH), e + 1, __ATOMIC_RELAXED, __HIP_MEMORY_SCOPE_AGENT);
    }
  }
}
}

extern "C" void kernel_launch(void* const* d_in, const int* in_sizes, int n_in, void* d_out, int out_size, void* d_ws, size_t ws_size,
                              hipStream_t stream) {
  dk::Params p;
  p.x = (const dk::u16*)d_in[0];
  p.kc = (const dk::u16*)d_in[1];
  p.vc = (const dk::u16*)d_in[2];
  p.wq = (const dk::u16*)d_in[3];
  p.wo = (const dk::u16*)d_in[4];
  p.wr = (const dk::u16*)d_in[6];
  p.wg = (const dk::u16*)d_in[7];
  p.wu = (const dk::u16*)d_in[8];
  p.wd = (const dk::u16*)d_in[9];
  p.out = (dk::u16*)d_out;
  p.ws = (unsigned char*)d_ws;
  hipLaunchKernelGGL(dk::fused_decode, dim3(256), dim3(512), 0, stream, p);
}
```
